```python
import torch

def _softmax(x, axis=-1):
    return torch.softmax(x, dim=axis)
def _gelu(x):
    return torch.nn.functional.gelu(x, approximate="tanh")
def _silu(x):
    return torch.nn.functional.silu(x)
def _sigmoid(x):
    return torch.sigmoid(x)
def _layernorm(x, axis=-1, eps=1e-5):
    m = x.mean(dim=axis, keepdim=True)
    v = x.var(dim=axis, keepdim=True, unbiased=False)
    return (x - m) / torch.sqrt(v + eps)
def _scatter_add(tgt, idx, src, axis=0):
    return tgt.clone().index_add_(axis, idx, src)
def _segment_sum(src, seg, n):
    out = torch.zeros((n,) + src.shape[1:], dtype=src.dtype)
    return out.index_add_(0, seg, src)
def _pad(x, pw):
    flat = tuple(v for pair in reversed(pw) for v in pair)
    return torch.nn.functional.pad(x, flat)

kernel_name = 'standalone__mixer_s_mlp__prompt'

def setup_inputs():

    _w = (lambda a: (lambda t: t.to(torch.bfloat16) if t.is_floating_point() else t)(torch.as_tensor(a)))
    B, S, D, F = 1, 2048, 1024, 3584
    return {
        "x":  _w((lambda *s: torch.randn(*s, dtype=torch.bfloat16) * 0.4)(B, S, D)),
        "w1": _w((lambda *s: torch.randn(*s, dtype=torch.bfloat16) * 3.2)(D, D) * (D ** -0.5)),
        "w2": _w((lambda *s: torch.randn(*s, dtype=torch.bfloat16) * 3.2)(D, D) * (D ** -0.5)),
        "w3": _w((lambda *s: torch.randn(*s, dtype=torch.bfloat16) * 3.2)(D, F) * (D ** -0.5)),
        "w4": _w((lambda *s: torch.randn(*s, dtype=torch.bfloat16) * 0.8)(F, D) * (F ** -0.5)),
    }

def reference(x, w1, w2, w3, w4):
    B, S, D = x.shape
    h = _gelu(_layernorm(x, axis=-1) @ w1)
    c = D // 4
    g0 = _pad(h[:, :, 0*c:1*c], ((0, 0), (1, 0), (0, 0)))[:, :S, :]
    g1 = _pad(h[:, :, 1*c:2*c], ((0, 0), (0, 1), (0, 0)))[:, 1:, :]
    g2 = _pad(h[:, :, 2*c:3*c], ((0, 0), (2, 0), (0, 0)))[:, :S, :]
    g3 = _pad(h[:, :, 3*c:   ], ((0, 0), (0, 2), (0, 0)))[:, 2:, :]
    hs = torch.concatenate([g0, g1, g2, g3], axis=-1) @ w2
    x1 = x + hs
    h2 = _gelu(_layernorm(x1, axis=-1) @ w3) @ w4
    return x1 + h2
```

```cpp
#include <hip/hip_runtime.h>
#include <hip/hip_bf16.h>
#include <stdint.h>

typedef unsigned short u16;
typedef __attribute__((ext_vector_type(8))) short bf16x8;
typedef __attribute__((ext_vector_type(4))) short s16x4;
typedef __attribute__((ext_vector_type(4))) float f32x4;
typedef __attribute__((ext_vector_type(2))) float f32x2;
typedef __attribute__((ext_vector_type(4))) unsigned u32x4;
typedef __attribute__((ext_vector_type(2))) __bf16 bf16x2_t;
#define LDS_AS __attribute__((address_space(3)))
typedef LDS_AS char* lds_ptr;

constexpr int S_ = 2048, D_ = 1024, F_ = 3584;
constexpr int BK = 64;

constexpr size_t WS_HS = 0;
constexpr size_t WS_X1 = WS_HS + (size_t)S_ * D_ * 2;
constexpr size_t WS_H2 = WS_X1 + (size_t)S_ * D_ * 2;
constexpr size_t WS_ST = WS_H2 + (size_t)S_ * F_ * 2;
constexpr size_t WS_CS = WS_ST + (size_t)S_ * 16 * 2 * 4;

__device__ __forceinline__ unsigned pack_bf16(float a, float b) {
  f32x2 v = {a, b};
  bf16x2_t r = __builtin_convertvector(v, bf16x2_t);
  return __builtin_bit_cast(unsigned, r);
}
__device__ __forceinline__ float bf_lo(unsigned u) { return __builtin_bit_cast(float, u << 16); }
__device__ __forceinline__ float bf_hi(unsigned u) { return __builtin_bit_cast(float, u & 0xffff0000u); }

__device__ __forceinline__ float gelu_tanh(float y) {
  constexpr float k0 = -2.0f * 0.7978845608028654f * 1.4426950408889634f;
  constexpr float k1 = k0 * 0.044715f;
  float z = y * (k0 + k1 * y * y);
  float e = __builtin_amdgcn_exp2f(z);
  return y * __builtin_amdgcn_rcpf(1.0f + e);
}

__device__ __forceinline__ f32x4 sel4(int c, f32x4 a, f32x4 b) {
  return f32x4{c ? a[0] : b[0], c ? a[1] : b[1], c ? a[2] : b[2], c ? a[3] : b[3]};
}
#define WAIT_VM(n) asm volatile("s_waitcnt vmcnt(%0)" ::"i"(n) : "memory")
#define BAR() asm volatile("s_barrier" ::: "memory")
#define LGKM0_BAR() asm volatile("s_waitcnt lgkmcnt(0)\n\ts_barrier" ::: "memory")

template <int MODE> struct Cfg;
template <> struct Cfg<1> { static constexpr int BN = 64,  K = 1024, LDA = 1024, LDB = 1024, LDC = 1024, NS = 6, WN = 1, WK = 2; };
template <> struct Cfg<2> { static constexpr int BN = 64,  K = 1024, LDA = 1024, LDB = 1024, LDC = 1024, NS = 5, WN = 1, WK = 2; };
template <> struct Cfg<3> { static constexpr int BN = 224, K = 1024, LDA = 1024, LDB = 3584, LDC = 3584, NS = 3, WN = 2, WK = 1; };
template <> struct Cfg<4> { static constexpr int BN = 64,  K = 3584, LDA = 3584, LDB = 1024, LDC = 1024, NS = 6, WN = 1, WK = 2; };

constexpr int BM = 128;

template <int MODE>
__global__ __launch_bounds__(512, 2) void gemm_k(const u16* __restrict__ A, const u16* __restrict__ B, u16* __restrict__ C,
                                                 const u16* __restrict__ R, float* __restrict__ stats,
                                                 float* __restrict__ csum, const u16* __restrict__ W3) {
#if defined(__HIP_DEVICE_COMPILE__)
  using G = Cfg<MODE>;
  constexpr int BN = G::BN, K = G::K, LDA = G::LDA, LDB = G::LDB, LDC = G::LDC, NS = G::NS, WN = G::WN, WK = G::WK;
  constexpr int NT = K / BK;
  constexpr int A_BYTES = BM * BK * 2, B_BYTES = BK * BN * 2, STG = A_BYTES + B_BYTES;
  constexpr int CPR = BN / 8;
  constexpr int LA = 4, LB = BN / 32, L = LA + LB;
  constexpr int NF = BN / WN / 16;
  constexpr int MF = 4;
  constexpr int NSUB = 2 / WK;
  extern __shared__ __attribute__((aligned(16))) char smem_raw[];
  lds_ptr smem = (lds_ptr)smem_raw;
  constexpr int XTRA = NS * STG;

  const int tid = threadIdx.x, lane = tid & 63;
  const int wave = __builtin_amdgcn_readfirstlane(tid >> 6);
  int bm, bn;
  {
    int b = blockIdx.x, x = b & 7, i = b >> 3;
    if (MODE == 3) { bm = (x >> 2) * 8 + (i & 7); bn = (x & 3) * 4 + (i >> 3); }
    else           { bm = (x >> 1) * 4 + (i & 3); bn = (x & 1) * 8 + (i >> 2); }
  }
  const int row0 = bm * BM, col0 = bn * BN;

  if (wave >= 4) {
    const int w = wave - 4;
    auto rsA = __builtin_amdgcn_make_buffer_rsrc((void*)(A + (size_t)row0 * LDA), (short)0, BM * LDA * 2, 0x00020000);
    auto rsB = __builtin_amdgcn_make_buffer_rsrc((void*)(B + col0), (short)0, (K * LDB - col0) * 2, 0x00020000);
    int voA;
    { int r = w * 8 + (lane >> 3); voA = r * LDA * 2 + (((lane & 7) ^ ((r >> 1) & 7)) << 4); }
    int voB[LB];
#pragma unroll
    for (int i = 0; i < LB; ++i) {
      int p = (w + 4 * i) * 64 + lane, r = p / CPR, pc = p % CPR;
      int sw = (BN == 64) ? ((((r >> 1) & 1) | (((r >> 3) & 1) << 1)) << 1) : (((r >> 3) & 1) << 1);
      voB[i] = r * LDB * 2 + ((pc ^ sw) << 4);
    }
    auto issue = [&](int t, int slot) {
      lds_ptr base = smem + slot * STG;
#pragma unroll
      for (int i = 0; i < LA; ++i)
        __builtin_amdgcn_raw_ptr_buffer_load_lds(rsA, (LDS_AS void*)(base + (w + 4 * i) * 1024), 16, voA, t * (BK * 2) + i * (32 * LDA * 2), 0, 0);
#pragma unroll
      for (int i = 0; i < LB; ++i)
        __builtin_amdgcn_raw_ptr_buffer_load_lds(rsB, (LDS_AS void*)(base + A_BYTES + (w + 4 * i) * 1024), 16, voB[i], t * (BK * LDB * 2), 0, 0);
    };
#pragma unroll
    for (int s = 0; s < NS; ++s) issue(s, s);
    constexpr int XL = (MODE == 2) ? 8 : 0;
    if (MODE == 2) {
      int b = blockIdx.x < 224 ? blockIdx.x : 0;
      auto rsW = __builtin_amdgcn_make_buffer_rsrc((void*)(W3 + (size_t)(b / 28) * 128 * F_ + (b % 28) * 128), (short)0, (127 * F_ + 128) * 2, 0x00020000);
      int vo = (w * 32 + (lane >> 4)) * F_ * 2 + (lane & 15) * 16;
#pragma unroll
      for (int i = 0; i < 8; ++i)
        __builtin_amdgcn_raw_ptr_buffer_load_lds(rsW, (LDS_AS void*)(smem + XTRA + (w * 8 + i) * 1024), 16, vo, i * 4 * F_ * 2, 0, 0);
    }
    WAIT_VM(L * (NS - 1) + XL);
    BAR();
    int slot = 0;
    for (int t = 0; t < NT - NS; ++t) {
      if (XL != 0 && t < NS - 1) WAIT_VM(L * (NS - 2) + XL); else WAIT_VM(L * (NS - 2));
      BAR();
      issue(t + NS, slot);
      slot = (slot + 1 == NS) ? 0 : slot + 1;
    }
#pragma unroll
    for (int r = NS - 2; r >= 0; --r) {
      if (r == 4) WAIT_VM(L * 4); else if (r == 3) WAIT_VM(L * 3); else if (r == 2) WAIT_VM(L * 2); else if (r == 1) WAIT_VM(L); else WAIT_VM(0);
      BAR();
    }
    if (MODE == 2) {
      float s0 = 0.f, s1 = 0.f;
#pragma unroll
      for (int r = 0; r < 32; ++r) {
        unsigned u = *(LDS_AS unsigned*)(smem + XTRA + (w * 32 + r) * 256 + lane * 4);
        s0 += bf_lo(u); s1 += bf_hi(u);
      }
      *(LDS_AS f32x2*)(smem + XTRA + w * 8192 + lane * 8) = f32x2{s0, s1};
    }
    if (WK == 2) { LGKM0_BAR(); }
    if (MODE == 2) {
      if (w == 0 && blockIdx.x < 224) {
        f32x2 t = {0.f, 0.f};
#pragma unroll
        for (int i = 0; i < 4; ++i) t += *(LDS_AS f32x2*)(smem + XTRA + i * 8192 + lane * 8);
        *(f32x2*)(csum + (blockIdx.x / 28) * F_ + (blockIdx.x % 28) * 128 + lane * 2) = t;
      }
    }
    if (WK == 2) { LGKM0_BAR(); }
    if (MODE == 3) {   }
    return;
  }

  const int wm = wave & 1, wn = (WN == 2) ? (wave >> 1) : 0, wk = (WK == 2) ? (wave >> 1) : 0;
  const int g = lane >> 4, fr = lane & 15;
  __builtin_amdgcn_s_setprio(1);

  float mean[MF], rstd[MF];
  u32x4 res[MF][2];
  if (MODE == 3) {
#pragma unroll
    for (int mf = 0; mf < MF; ++mf) {
      const f32x4* p = (const f32x4*)(stats + (size_t)(row0 + wm * 64 + mf * 16 + fr) * 32 + g * 8);
      f32x4 a = p[0], b = p[1];
      float s = a[0] + a[2] + b[0] + b[2], q = a[1] + a[3] + b[1] + b[3];
      s += __shfl_xor(s, 16); q += __shfl_xor(q, 16);
      s += __shfl_xor(s, 32); q += __shfl_xor(q, 32);
      float m = s * (1.0f / D_), v = q * (1.0f / D_) - m * m;
      mean[mf] = m; rstd[mf] = __builtin_amdgcn_rsqf(fmaxf(v, 0.f) + 1e-5f);
    }
    if (tid < BN) {
      float s = 0.f;
#pragma unroll
      for (int i = 0; i < 8; ++i) s += csum[i * F_ + col0 + tid];
      *(LDS_AS float*)(smem + XTRA + tid * 4) = s;
    }
  }
  if (MODE == 2 || MODE == 4) {
#pragma unroll
    for (int mf = 0; mf < MF; ++mf)
      res[mf][0] = *(const u32x4*)(R + (size_t)(row0 + wm * 64 + mf * 16 + fr) * 1024 + col0 + wk * 32 + (g & 1) * 16 + (g >> 1) * 8);
  }

  f32x4 acc[MF][NF];
#pragma unroll
  for (int i = 0; i < MF; ++i)
#pragma unroll
    for (int j = 0; j < NF; ++j) acc[i][j] = f32x4{0.f, 0.f, 0.f, 0.f};
  f32x4 rs[MF], sq[MF], cs[2];
#pragma unroll
  for (int i = 0; i < MF; ++i) { rs[i] = f32x4{0.f, 0.f, 0.f, 0.f}; sq[i] = f32x4{0.f, 0.f, 0.f, 0.f}; }
  cs[0] = cs[1] = f32x4{0.f, 0.f, 0.f, 0.f};
  const bf16x8 ones = {0x3f80, 0x3f80, 0x3f80, 0x3f80, 0x3f80, 0x3f80, 0x3f80, 0x3f80};

  int aoff[2];
#pragma unroll
  for (int kk = 0; kk < 2; ++kk) aoff[kk] = (wm * 64 + fr) * 128 + (((((WK == 2) ? wk : kk) * 4 + g) ^ ((fr >> 1) & 7)) << 4);
  const int q = fr >> 2, p = fr & 3;
  const int swB = (BN == 64) ? (((q >> 1) & 1) | ((g & 1) << 1)) : (g & 1);
  int boff[NF];
#pragma unroll
  for (int nf = 0; nf < NF; ++nf) boff[nf] = A_BYTES + (8 * g + q + wk * 32) * (BN * 2) + (((wn * NF + nf) ^ swB) << 5) + p * 8;

  bf16x8 xa[2][MF], wb[2][NF];
  auto rd = [&](int buf, int slot, int kk) {
    lds_ptr base = smem + slot * STG;
#pragma unroll
    for (int mf = 0; mf < MF; ++mf) xa[buf][mf] = *(LDS_AS bf16x8*)(base + aoff[kk] + mf * 2048);
#pragma unroll
    for (int nf = 0; nf < NF; ++nf) {
      s16x4 lo = __builtin_amdgcn_ds_read_tr16_b64_v4i16((LDS_AS s16x4*)(base + boff[nf] + kk * 32 * BN * 2));
      s16x4 hi = __builtin_amdgcn_ds_read_tr16_b64_v4i16((LDS_AS s16x4*)(base + boff[nf] + kk * 32 * BN * 2 + 4 * BN * 2));
      wb[buf][nf] = __builtin_shufflevector(lo, hi, 0, 1, 2, 3, 4, 5, 6, 7);
    }
  };
  auto mma = [&](int buf) {
#pragma unroll
    for (int nf = 0; nf < NF; ++nf)
#pragma unroll
      for (int mf = 0; mf < MF; ++mf)
        acc[mf][nf] = __builtin_amdgcn_mfma_f32_16x16x32_bf16(wb[buf][nf], xa[buf][mf], acc[mf][nf], 0, 0, 0);
    if (MODE == 1) {
#pragma unroll
      for (int mf = 0; mf < MF; ++mf) {
        rs[mf] = __builtin_amdgcn_mfma_f32_16x16x32_bf16(ones, xa[buf][mf], rs[mf], 0, 0, 0);
        sq[mf] = __builtin_amdgcn_mfma_f32_16x16x32_bf16(xa[buf][mf], xa[buf][mf], sq[mf], 0, 0, 0);
      }
      if (wm) {
        cs[0] = __builtin_amdgcn_mfma_f32_16x16x32_bf16(wb[buf][2 % NF], ones, cs[0], 0, 0, 0);
        cs[1] = __builtin_amdgcn_mfma_f32_16x16x32_bf16(wb[buf][3 % NF], ones, cs[1], 0, 0, 0);
      } else {
        cs[0] = __builtin_amdgcn_mfma_f32_16x16x32_bf16(wb[buf][0], ones, cs[0], 0, 0, 0);
        cs[1] = __builtin_amdgcn_mfma_f32_16x16x32_bf16(wb[buf][1], ones, cs[1], 0, 0, 0);
      }
    }
  };

  LGKM0_BAR();
  if (WK == 2) {
    rd(0, 0, 0);
    int slot = 1;
    for (int t = 0; t < NT - 2; t += 2) {
      LGKM0_BAR();
      rd(1, slot, 0); slot = (slot + 1 == NS) ? 0 : slot + 1;
      mma(0);
      LGKM0_BAR();
      rd(0, slot, 0); slot = (slot + 1 == NS) ? 0 : slot + 1;
      mma(1);
    }
    LGKM0_BAR();
    rd(1, slot, 0);
    mma(0);
    mma(1);
  } else {
    rd(0, 0, 0);
    int slot = 0;
    for (int t = 0; t < NT - 1; ++t) {
      rd(1, slot, 1);
      mma(0);
      LGKM0_BAR();
      slot = (slot + 1 == NS) ? 0 : slot + 1;
      rd(0, slot, 0);
      mma(1);
    }
    rd(1, slot, 1);
    mma(0);
    mma(1);
  }
  __builtin_amdgcn_s_setprio(0);

  if (WK == 2) {
    LGKM0_BAR();
    lds_ptr xb = smem + (wave * 8) * 1024;
#pragma unroll
    for (int mf = 0; mf < MF; ++mf)
#pragma unroll
      for (int j = 0; j < 2; ++j)
        *(LDS_AS f32x4*)(xb + (mf * 2 + j) * 1024 + lane * 16) = sel4(wk, acc[mf][j], acc[mf][(2 + j) % NF]);
    LDS_AS float* st = (LDS_AS float*)(smem + 32768);
    if (MODE == 1) {
#pragma unroll
      for (int mf = 0; mf < MF; ++mf) {
        float d = (fr & 3) == 0 ? sq[mf][0] : (fr & 3) == 1 ? sq[mf][1] : (fr & 3) == 2 ? sq[mf][2] : sq[mf][3];
        if (g == (fr >> 2)) { st[(wk * 128 + wm * 64 + mf * 16 + fr) * 2] = rs[mf][0]; st[(wk * 128 + wm * 64 + mf * 16 + fr) * 2 + 1] = d; }
      }
      if (fr == 0) {
        *(LDS_AS f32x4*)(st + 512 + wk * 64 + wm * 32 + g * 4) = cs[0];
        *(LDS_AS f32x4*)(st + 512 + wk * 64 + wm * 32 + 16 + g * 4) = cs[1];
      }
    }
    LGKM0_BAR();
    lds_ptr pb = smem + ((wave ^ 2) * 8) * 1024;
    f32x4 v[MF][2];
#pragma unroll
    for (int mf = 0; mf < MF; ++mf)
#pragma unroll
      for (int j = 0; j < 2; ++j)
        v[mf][j] = sel4(wk, acc[mf][(2 + j) % NF], acc[mf][j]) + *(LDS_AS f32x4*)(pb + (mf * 2 + j) * 1024 + lane * 16);
    if (MODE == 1) {
      f32x4 c[2];
#pragma unroll
      for (int j = 0; j < 2; ++j)
        c[j] = *(LDS_AS f32x4*)(st + 512 + wk * 32 + j * 16 + g * 4) + *(LDS_AS f32x4*)(st + 512 + 64 + wk * 32 + j * 16 + g * 4);
      const int grp = col0 >> 8, dl = (grp == 0) ? 1 : (grp == 1) ? -1 : (grp == 2) ? 2 : -2;
#pragma unroll
      for (int mf = 0; mf < MF; ++mf) {
        int r = wm * 64 + mf * 16 + fr;
        f32x2 a = *(LDS_AS f32x2*)(st + r * 2), b = *(LDS_AS f32x2*)(st + (128 + r) * 2);
        float m = (a[0] + b[0]) * (1.0f / D_), var = (a[1] + b[1]) * (1.0f / D_) - m * m;
        float rsd = __builtin_amdgcn_rsqf(fmaxf(var, 0.f) + 1e-5f);
        unsigned pk[2][2];
#pragma unroll
        for (int j = 0; j < 2; ++j) {
          float y0 = gelu_tanh((v[mf][j][0] - m * c[j][0]) * rsd), y1 = gelu_tanh((v[mf][j][1] - m * c[j][1]) * rsd);
          float y2 = gelu_tanh((v[mf][j][2] - m * c[j][2]) * rsd), y3 = gelu_tanh((v[mf][j][3] - m * c[j][3]) * rsd);
          pk[j][0] = pack_bf16(y0, y1); pk[j][1] = pack_bf16(y2, y3);
        }
        auto s0 = __builtin_amdgcn_permlane16_swap(pk[0][0], pk[1][0], false, false);
        auto s1 = __builtin_amdgcn_permlane16_swap(pk[0][1], pk[1][1], false, false);
        u32x4 o = {s0[0], s1[0], s0[1], s1[1]};
        int dr = row0 + r + dl;
        if (dr < 0 || dr >= S_) { o = u32x4{0u, 0u, 0u, 0u}; dr &= (S_ - 1); }
        *(u32x4*)(C + (size_t)dr * LDC + col0 + wk * 32 + (g & 1) * 16 + (g >> 1) * 8) = o;
      }
    } else {
      LDS_AS float* sp = (LDS_AS float*)(smem + 40960);
#pragma unroll
      for (int mf = 0; mf < MF; ++mf) {
        float y[8];
#pragma unroll
        for (int e = 0; e < 4; ++e) {
          float f0 = v[mf][0][e], f1 = v[mf][1][e];
          auto s = __builtin_amdgcn_permlane16_swap(__float_as_uint(f0), __float_as_uint(f1), false, false);
          unsigned u0 = s[0], u1 = s[1];
          y[e] = __uint_as_float(u0); y[4 + e] = __uint_as_float(u1);
        }
        u32x4 o;
#pragma unroll
        for (int e = 0; e < 4; ++e) o[e] = pack_bf16(y[2 * e] + bf_lo(res[mf][0][e]), y[2 * e + 1] + bf_hi(res[mf][0][e]));
        int r = wm * 64 + mf * 16 + fr;
        *(u32x4*)(C + (size_t)(row0 + r) * LDC + col0 + wk * 32 + (g & 1) * 16 + (g >> 1) * 8) = o;
        if (MODE == 2) {
          float s = 0.f, qq = 0.f;
#pragma unroll
          for (int e = 0; e < 4; ++e) { float a = bf_lo(o[e]), b = bf_hi(o[e]); s += a + b; qq += a * a + b * b; }
          s += __shfl_xor(s, 16); qq += __shfl_xor(qq, 16);
          s += __shfl_xor(s, 32); qq += __shfl_xor(qq, 32);
          if (g == 0) *(LDS_AS f32x2*)(sp + (wk * 128 + r) * 2) = f32x2{s, qq};
        }
      }
      if (MODE == 2) {
        LGKM0_BAR();
        if (tid < 128) {
          f32x2 a = *(LDS_AS f32x2*)(sp + tid * 2), b = *(LDS_AS f32x2*)(sp + (128 + tid) * 2);
          *(f32x2*)(stats + (size_t)(row0 + tid) * 32 + bn * 2) = a + b;
        }
      }
    }
  } else {
#pragma unroll
    for (int mf = 0; mf < MF; ++mf) {
      const float m = mean[mf], rsd = rstd[mf];
      u16* crow = C + (size_t)(row0 + wm * 64 + mf * 16 + fr) * LDC + col0 + wn * (NF * 16);
      unsigned pk[NF][2];
#pragma unroll
      for (int nf = 0; nf < NF; ++nf) {
        f32x4 c = *(LDS_AS f32x4*)(smem + XTRA + ((wn * NF + nf) * 16 + g * 4) * 4);
        float y0 = gelu_tanh((acc[mf][nf][0] - m * c[0]) * rsd), y1 = gelu_tanh((acc[mf][nf][1] - m * c[1]) * rsd);
        float y2 = gelu_tanh((acc[mf][nf][2] - m * c[2]) * rsd), y3 = gelu_tanh((acc[mf][nf][3] - m * c[3]) * rsd);
        pk[nf][0] = pack_bf16(y0, y1); pk[nf][1] = pack_bf16(y2, y3);
      }
#pragma unroll
      for (int j = 0; j + 1 < NF; j += 2) {
        auto s0 = __builtin_amdgcn_permlane16_swap(pk[j][0], pk[j + 1][0], false, false);
        auto s1 = __builtin_amdgcn_permlane16_swap(pk[j][1], pk[j + 1][1], false, false);
        *(u32x4*)(crow + j * 16 + (g & 1) * 16 + (g >> 1) * 8) = u32x4{s0[0], s1[0], s0[1], s1[1]};
      }
      if (NF & 1) *(uint2*)(crow + (NF - 1) * 16 + g * 4) = uint2{pk[NF - 1][0], pk[NF - 1][1]};
    }
  }
#endif
}

template <int MODE> constexpr int lds_bytes() {
  return Cfg<MODE>::NS * (BM * BK * 2 + BK * Cfg<MODE>::BN * 2) + (MODE == 2 ? 32768 : MODE == 3 ? 1024 : 0);
}

extern "C" void kernel_launch(void* const* d_in, const int* in_sizes, int n_in, void* d_out, int out_size, void* d_ws,
                              size_t ws_size, hipStream_t stream) {
  const u16* x = (const u16*)d_in[0];
  const u16* w1 = (const u16*)d_in[1];
  const u16* w2 = (const u16*)d_in[2];
  const u16* w3 = (const u16*)d_in[3];
  const u16* w4 = (const u16*)d_in[4];
  char* ws = (char*)d_ws;
  u16* hs = (u16*)(ws + WS_HS);
  u16* x1 = (u16*)(ws + WS_X1);
  u16* h2 = (u16*)(ws + WS_H2);
  float* st = (float*)(ws + WS_ST);
  float* cs = (float*)(ws + WS_CS);
  (void)hipFuncSetAttribute((const void*)gemm_k<1>, hipFuncAttributeMaxDynamicSharedMemorySize, lds_bytes<1>());
  (void)hipFuncSetAttribute((const void*)gemm_k<2>, hipFuncAttributeMaxDynamicSharedMemorySize, lds_bytes<2>());
  (void)hipFuncSetAttribute((const void*)gemm_k<3>, hipFuncAttributeMaxDynamicSharedMemorySize, lds_bytes<3>());
  (void)hipFuncSetAttribute((const void*)gemm_k<4>, hipFuncAttributeMaxDynamicSharedMemorySize, lds_bytes<4>());
#define KMASK 15
  if (KMASK & 1) gemm_k<1><<<256, 512, lds_bytes<1>(), stream>>>(x, w1, hs, nullptr, nullptr, nullptr, nullptr);
  if (KMASK & 2) gemm_k<2><<<256, 512, lds_bytes<2>(), stream>>>(hs, w2, x1, x, st, cs, w3);
  if (KMASK & 4) gemm_k<3><<<256, 512, lds_bytes<3>(), stream>>>(x1, w3, h2, nullptr, st, cs, nullptr);
  if (KMASK & 8) gemm_k<4><<<256, 512, lds_bytes<4>(), stream>>>(h2, w4, (u16*)d_out, x1, nullptr, nullptr, nullptr);
}
```

```cpp
#include <hip/hip_runtime.h>
#include <hip/hip_bf16.h>
#include <stdint.h>
#include <type_traits>

typedef unsigned short u16;
typedef __attribute__((ext_vector_type(8))) short bf16x8;
typedef __attribute__((ext_vector_type(4))) short s16x4;
typedef __attribute__((ext_vector_type(4))) float f32x4;
typedef __attribute__((ext_vector_type(2))) float f32x2;
typedef __attribute__((ext_vector_type(4))) unsigned u32x4;
typedef __attribute__((ext_vector_type(2))) __bf16 bf16x2_t;
#define LDS_AS __attribute__((address_space(3)))
typedef LDS_AS char* lds_ptr;

constexpr int S_ = 2048, D_ = 1024, F_ = 3584;
constexpr int BK = 64;

constexpr size_t WS_HS = 0;
constexpr size_t WS_X1 = WS_HS + (size_t)S_ * D_ * 2;
constexpr size_t WS_H2 = WS_X1 + (size_t)S_ * D_ * 2;
constexpr size_t WS_ST = WS_H2 + (size_t)S_ * F_ * 2;
constexpr size_t WS_CS = WS_ST + (size_t)S_ * 32 * 2 * 4;

__device__ __forceinline__ unsigned pack_bf16(float a, float b) {
  f32x2 v = {a, b};
  bf16x2_t r = __builtin_convertvector(v, bf16x2_t);
  return __builtin_bit_cast(unsigned, r);
}
__device__ __forceinline__ float bf_lo(unsigned u) { return __builtin_bit_cast(float, u << 16); }
__device__ __forceinline__ float bf_hi(unsigned u) { return __builtin_bit_cast(float, u & 0xffff0000u); }

__device__ __forceinline__ float gelu_tanh(float y) {
  constexpr float k0 = -2.0f * 0.7978845608028654f * 1.4426950408889634f;
  constexpr float k1 = k0 * 0.044715f;
  float z = y * (k0 + k1 * y * y);
  float e = __builtin_amdgcn_exp2f(z);
  return y * __builtin_amdgcn_rcpf(1.0f + e);
}

__device__ __forceinline__ unsigned gelu2_pack(f32x2 y) {
  constexpr float k0 = -2.0f * 0.7978845608028654f * 1.4426950408889634f;
  constexpr float k1 = k0 * 0.044715f;
  f32x2 z = y * (y * y * k1 + k0);
  f32x2 e = {__builtin_amdgcn_exp2f(z[0]), __builtin_amdgcn_exp2f(z[1])};
  e += 1.0f;
  f32x2 r = {__builtin_amdgcn_rcpf(e[0]), __builtin_amdgcn_rcpf(e[1])};
  y *= r;
  return pack_bf16(y[0], y[1]);
}
__device__ __forceinline__ float xsum16(float v) {
  auto s = __builtin_amdgcn_permlane16_swap(__float_as_uint(v), __float_as_uint(v), false, false);
  unsigned a = s[0], b = s[1];
  return __uint_as_float(a) + __uint_as_float(b);
}
__device__ __forceinline__ float xsum32(float v) {
  auto s = __builtin_amdgcn_permlane32_swap(__float_as_uint(v), __float_as_uint(v), false, false);
  unsigned a = s[0], b = s[1];
  return __uint_as_float(a) + __uint_as_float(b);
}
#define SB0() __builtin_amdgcn_sched_barrier(0)
template <int N> __device__ __forceinline__ void wait_vm() { asm volatile("s_waitcnt vmcnt(%0)" ::"i"(N) : "memory"); }
__device__ __forceinline__ int vadd(int a, int s) { int r; asm volatile("v_add_u32 %0, %1, %2" : "=v"(r) : "s"(s), "v"(a)); return r; }
#define STAUX 0
#define TSTAMP(i)
#define WAIT_VM(n) asm volatile("s_waitcnt vmcnt(%0)" ::"i"(n) : "memory")
#define BAR() asm volatile("s_barrier" ::: "memory")
#define LGKM0() __builtin_amdgcn_s_waitcnt(0xC07F)
#define LGKM0_BAR() do { LGKM0(); asm volatile("s_barrier" ::: "memory"); } while (0)

template <int MODE> struct Cfg;
template <> struct Cfg<1> { static constexpr int BN = 64,  K = 1024, LDA = 1024, LDB = 1024, LDC = 1024, NS = 4; };
template <> struct Cfg<2> { static constexpr int BN = 64,  K = 1024, LDA = 1024, LDB = 1024, LDC = 1024, NS = 4; };
template <> struct Cfg<3> { static constexpr int BN = 224, K = 1024, LDA = 1024, LDB = 3584, LDC = 3584, NS = 3; };
template <> struct Cfg<4> { static constexpr int BN = 64,  K = 3584, LDA = 3584, LDB = 1024, LDC = 1024, NS = 4; };
constexpr int BM = 128;
constexpr int cmin(int a, int b) { return a < b ? a : b; }
constexpr int ramp(int t, int NS, int NT) { return t == 0 ? 2 : cmin(cmin(ramp(t - 1, NS, NT) + 2, t - 1 + NS), NT); }

template <int MODE>
__global__ __launch_bounds__(512, 2) void gemm_k(const u16* __restrict__ A, const u16* __restrict__ B, u16* __restrict__ C,
                                                 const u16* __restrict__ R, float* __restrict__ stats,
                                                 float* __restrict__ csum, const u16* __restrict__ P0, const u16* __restrict__ P1) {
#if defined(__HIP_DEVICE_COMPILE__)
  using G = Cfg<MODE>;
  constexpr int BN = G::BN, K = G::K, LDA = G::LDA, LDB = G::LDB, LDC = G::LDC, NS = G::NS;
  constexpr bool KSPLIT = (MODE != 3);
  constexpr int NT = K / BK;
  constexpr int A_BYTES = BM * BK * 2, B_BYTES = BK * BN * 2, STG = A_BYTES + B_BYTES;
  constexpr int CPR = BN / 8;
  constexpr int LA = 4, LB = BN / 32, L = LA + LB;
  constexpr int NF = KSPLIT ? 4 : 7, MF = 4;
  extern __shared__ __attribute__((aligned(16))) char smem_raw[];
  lds_ptr smem = (lds_ptr)smem_raw;
  constexpr int XTRA = NS * STG;

  const int tid = threadIdx.x, lane = tid & 63;
  const int wave = __builtin_amdgcn_readfirstlane(tid >> 6);
  const int xcd = blockIdx.x & 7, bi = blockIdx.x >> 3;
  const int bm = (MODE == 3) ? (xcd >> 2) * 8 + (bi & 7) : (xcd >> 1) * 4 + (bi & 3);
  const int bn = (MODE == 3) ? (xcd & 3) * 4 + (bi >> 3) : (xcd & 1) * 8 + (bi >> 2);
  const int row0 = bm * BM, col0 = bn * BN;
  TSTAMP(0);

  if (__builtin_expect(wave < 4, 1)) {
    const int w = wave;
#define KR2 0
    const int t0 = (((xcd * NT) >> 3) + (KR2)) % NT;
    auto rsA = __builtin_amdgcn_make_buffer_rsrc((void*)(A + (size_t)row0 * LDA), (short)0, BM * LDA * 2, 0x00020000);
    auto rsB = __builtin_amdgcn_make_buffer_rsrc((void*)(B + col0), (short)0, (K * LDB - col0) * 2, 0x00020000);
    const int rA = w * 8 + (lane >> 3);
    const int voA = rA * LDA * 2 + (((lane & 7) ^ ((rA >> 1) & 7)) << 4);
    int voB[LB];
    auto issueA = [&](int t, int slot) {
      t += t0; if (t >= NT) t -= NT;
#pragma unroll
      for (int i = 0; i < LA; ++i)
        __builtin_amdgcn_raw_ptr_buffer_load_lds(rsA, (LDS_AS void*)(smem + slot * STG + (w + 4 * i) * 1024), 16, voA, t * (BK * 2) + i * (32 * LDA * 2), 0, 0);
    };
    auto issueB = [&](int t, int slot) {
      t += t0; if (t >= NT) t -= NT;
#pragma unroll
      for (int i = 0; i < LB; ++i)
        __builtin_amdgcn_raw_ptr_buffer_load_lds(rsB, (LDS_AS void*)(smem + slot * STG + A_BYTES + (w + 4 * i) * 1024), 16, voB[i], t * (BK * LDB * 2), 0, 0);
    };
    issueA(0, 0);
    SB0();
#pragma unroll
    for (int i = 0; i < LB; ++i) {
      int p = (w + 4 * i) * 64 + lane, r = p / CPR, pc = p % CPR;
      int sw = (BN == 64) ? ((((r >> 1) & 1) | (((r >> 3) & 1) << 1)) << 1) : (((r >> 3) & 1) << 1);
      voB[i] = r * LDB * 2 + ((pc ^ sw) << 4);
    }
    issueB(0, 0);
    issueA(1, 1); issueB(1, 1);
    wait_vm<L>();
    BAR();
    auto rampstep = [&](auto tc) {
      constexpr int t = decltype(tc)::value, s0 = ramp(t - 1, NS, NT), s1 = ramp(t, NS, NT);
#pragma unroll
      for (int s = s0; s < s1; ++s) { issueA(s, s % NS); issueB(s, s % NS); }
      wait_vm<L * (s1 - (t + 1))>();
      BAR();
    };
    rampstep(std::integral_constant<int, 1>{});
    rampstep(std::integral_constant<int, 2>{});
    static_assert(ramp(2, NS, NT) == 1 + NS, "ramp");
    int slot = (1 + NS) % NS;
    for (int t = 3; t <= NT - NS + 1; ++t) {
      issueA(t - 2 + NS, slot); issueB(t - 2 + NS, slot);
      slot = (slot + 1 == NS) ? 0 : slot + 1;
      wait_vm<L * (NS - 2)>();
      BAR();
    }
    f32x4 sv[8]; float cv[16];
    constexpr int XL = (MODE == 3) ? 24 : (MODE == 4) ? 0 : 4;
    const int sjb = blockIdx.x < 224 ? blockIdx.x : 0;
    if (MODE == 1 || MODE == 2) {
      auto rsW = __builtin_amdgcn_make_buffer_rsrc((void*)(P0 + (size_t)((sjb / 28) * 128 + (MODE - 1) * 64) * F_ + (sjb % 28) * 128), (short)0, (63 * F_ + 128) * 2, 0x00020000);
      int vo = (lane >> 2) * F_ * 2 + w * 64 + (lane & 3) * 16;
#pragma unroll
      for (int i = 0; i < 4; ++i)
        __builtin_amdgcn_raw_ptr_buffer_load_lds(rsW, (LDS_AS void*)(smem + XTRA + 40960 + (w * 4 + i) * 1024), 16, vo, i * 16 * F_ * 2, 0, 0);
    }
    if (MODE == 3) {
      const f32x4* p = (const f32x4*)(stats + (size_t)(row0 + (tid >> 1)) * 64 + (tid & 1) * 32);
#pragma unroll
      for (int i = 0; i < 8; ++i) sv[i] = p[i];
      const int c = col0 + (tid < BN ? tid : 0);
#pragma unroll
      for (int i = 0; i < 16; ++i) cv[i] = csum[i * F_ + c];
    }
    if (NS == 4) { wait_vm<L + XL>(); BAR(); }
    wait_vm<XL>();
    BAR();
    if (MODE != 3) BAR();
    unsigned pfd = 0;
    if (MODE == 1 || MODE == 2) {
      if (MODE == 1 && w == 0) asm volatile("global_load_dword %0, %1, off" : "+v"(pfd) : "v"((const char*)P1 + (size_t)(blockIdx.x * 64 + lane) * 128) : "memory");
      wait_vm<0>();
      float s0 = 0.f, s1 = 0.f;
#pragma unroll
      for (int r = 0; r < 16; ++r) {
        unsigned u = *(LDS_AS unsigned*)(smem + XTRA + 40960 + w * 4096 + r * 256 + lane * 4);
        s0 += bf_lo(u); s1 += bf_hi(u);
      }
      s0 = xsum32(xsum16(s0)); s1 = xsum32(xsum16(s1));
      if (blockIdx.x < 224 && lane < 16)
        *(f32x2*)(csum + (size_t)((sjb / 28) * 2 + (MODE - 1)) * F_ + (sjb % 28) * 128 + w * 32 + lane * 2) = f32x2{s0, s1};
    }
    if (MODE == 3) {
      f32x4 a = (sv[0] + sv[1]) + (sv[2] + sv[3]) + ((sv[4] + sv[5]) + (sv[6] + sv[7]));
      float s = a[0] + a[2], q = a[1] + a[3];
      s += __shfl_xor(s, 1); q += __shfl_xor(q, 1);
      float m = s * (1.0f / D_), v = q * (1.0f / D_) - m * m;
      float rsd = __builtin_amdgcn_rsqf(fmaxf(v, 0.f) + 1e-5f);
      if ((tid & 1) == 0) *(LDS_AS f32x2*)(smem + XTRA + 1024 + (tid >> 1) * 8) = f32x2{rsd, -m * rsd};
      float c = 0.f;
#pragma unroll
      for (int i = 0; i < 16; ++i) c += cv[i];
      *(LDS_AS float*)(smem + XTRA + tid * 4) = c;
      if (tid < 224) asm volatile("global_load_dword %0, %1, off" : "+v"(pfd) : "v"((const char*)P1 + (size_t)(blockIdx.x * 224 + tid) * 128) : "memory");
      LGKM0_BAR();
    }
    asm volatile("s_waitcnt vmcnt(0)" : "+v"(pfd) :: "memory");
    return;
  }

  const int cw = wave - 4;
  const int wm = cw & 1, wx = cw >> 1;
  const int g = lane >> 4, fr = lane & 15;
  const int keep = wx ^ wm;
  __builtin_amdgcn_s_setprio(1);
  u32x4 res[MF];
  if (MODE == 2 || MODE == 4) {
#pragma unroll
    for (int mf = 0; mf < MF; ++mf)
      res[mf] = *(const u32x4*)(R + (size_t)(row0 + wm * 64 + mf * 16 + fr) * 1024 + col0 + keep * 32 + (g & 1) * 16 + (g >> 1) * 8);
  }
  f32x4 acc[MF][NF];
#pragma unroll
  for (int i = 0; i < MF; ++i)
#pragma unroll
    for (int j = 0; j < NF; ++j) acc[i][j] = f32x4{0.f, 0.f, 0.f, 0.f};
  f32x4 rs[MF], sq[MF], cs[2];
#pragma unroll
  for (int i = 0; i < MF; ++i) { rs[i] = f32x4{0.f, 0.f, 0.f, 0.f}; sq[i] = f32x4{0.f, 0.f, 0.f, 0.f}; }
  cs[0] = cs[1] = f32x4{0.f, 0.f, 0.f, 0.f};
  const bf16x8 ones = {0x3f80, 0x3f80, 0x3f80, 0x3f80, 0x3f80, 0x3f80, 0x3f80, 0x3f80};
  const int sbase = (int)(uintptr_t)smem;
  const int q = fr >> 2, p = fr & 3;
  auto ldA = [&](int addr, int off) { return *(LDS_AS bf16x8*)(uintptr_t)(addr + off); };
  auto ldT = [&](int addr, int off) { return __builtin_amdgcn_ds_read_tr16_b64_v4i16((LDS_AS s16x4*)(uintptr_t)(addr + off)); };
#define CAT(l, h) __builtin_shufflevector(l, h, 0, 1, 2, 3, 4, 5, 6, 7)
#define MFMA(a, b, c) __builtin_amdgcn_mfma_f32_16x16x32_bf16(a, b, c, 0, 0, 0)

  if constexpr (KSPLIT) {
    const int a0 = sbase + (wm * 64 + fr) * 128 + (((wx * 4 + g) ^ ((fr >> 1) & 7)) << 4);
    const int swB = ((q >> 1) & 1) | ((g & 1) << 1);
    int b0[4];
#pragma unroll
    for (int nf = 0; nf < 4; ++nf) b0[nf] = sbase + A_BYTES + (8 * g + q + wx * 32) * 128 + (((nf ^ (2 * keep)) ^ swB) << 5) + p * 8;
    bf16x8 xa[2][4]; s16x4 wl[2][4], wh[2][4];
    int pa = a0, pb[4] = {b0[0], b0[1], b0[2], b0[3]};
    int slot = 0;
    auto step = [&](int c, bool RD) {
      const int n = c ^ 1;
      int so = 0;
      if (RD) { slot = (slot + 1 == NS) ? 0 : slot + 1; so = slot * STG; }
#pragma unroll
      for (int i = 0; i < 16; ++i) {
        const int nf = i >> 2, mf = i & 3;
        acc[mf][nf] = MFMA(CAT(wl[c][nf], wh[c][nf]), xa[c][mf], acc[mf][nf]); SB0();
        if (RD) {
          if (i < 2) { xa[n][2 * i] = ldA(pa, 2 * i * 2048); xa[n][2 * i + 1] = ldA(pa, (2 * i + 1) * 2048); }
          else if (i < 6) { wl[n][i - 2] = ldT(pb[i - 2], 0); wh[n][i - 2] = ldT(pb[i - 2], 512); }
          else if (i == 6) pa = vadd(a0, so);
          else if (i < 11) pb[i - 7] = vadd(b0[i - 7], so);
          SB0();
        }
      }
      if (MODE == 1) {
#pragma unroll
        for (int mf = 0; mf < 4; ++mf) { rs[mf] = MFMA(ones, xa[c][mf], rs[mf]); sq[mf] = MFMA(xa[c][mf], xa[c][mf], sq[mf]); }
        cs[0] = MFMA(CAT(wl[c][0], wh[c][0]), ones, cs[0]);
        cs[1] = MFMA(CAT(wl[c][1], wh[c][1]), ones, cs[1]);
        SB0();
      }
    };
    LGKM0_BAR();
    TSTAMP(1);
#pragma unroll
    for (int i = 0; i < 4; ++i) { xa[0][i] = ldA(pa, i * 2048); wl[0][i] = ldT(pb[i], 0); wh[0][i] = ldT(pb[i], 512); }
    slot = 1; pa = a0 + STG;
#pragma unroll
    for (int i = 0; i < 4; ++i) pb[i] = b0[i] + STG;
    SB0();
#pragma unroll 1
    for (int t = 0; t < NT - 2; t += 2) {
      LGKM0_BAR(); SB0(); step(0, true);
      LGKM0_BAR(); SB0(); step(1, true);
    }
    LGKM0_BAR(); SB0(); step(0, true);
    step(1, false);
  } else {
    int a0[2], b0[2];
#pragma unroll
    for (int kk = 0; kk < 2; ++kk) a0[kk] = sbase + (wm * 64 + fr) * 128 + (((kk * 4 + g) ^ ((fr >> 1) & 7)) << 4);
#pragma unroll
    for (int e = 0; e < 2; ++e) b0[e] = sbase + A_BYTES + (8 * g + q) * (BN * 2) + ((((wx * NF + e) ^ (g & 1)) - e) << 5) + p * 8;
    bf16x8 xa[2][4]; s16x4 wl[NF], wh[NF];
    int pa[2] = {a0[0], a0[1]}, pb[2] = {b0[0], b0[1]};
    auto rdL = [&](int kk, int nf) { wl[nf] = ldT(pb[nf & 1], nf * 32 + kk * 32 * BN * 2); SB0(); };
    auto rdH = [&](int kk, int nf) { wh[nf] = ldT(pb[nf & 1], nf * 32 + kk * 32 * BN * 2 + 4 * BN * 2); SB0(); };
    auto rdA = [&](int kk, int mf) { xa[kk][mf] = ldA(pa[kk], mf * 2048); SB0(); };
    auto mm = [&](int kk, int nf, int mf) { acc[mf][nf] = MFMA(CAT(wl[nf], wh[nf]), xa[kk][mf], acc[mf][nf]); SB0(); };
    auto grp = [&](int kk, int nf, int pk, int pn, int amf) {
      mm(kk, nf, 0); if (pn >= 0) rdL(pk, pn);
      mm(kk, nf, 1); if (pn >= 0) rdH(pk, pn);
      mm(kk, nf, 2); if (amf >= 0 && amf < MF) rdA(kk ^ 1, amf);
      mm(kk, nf, 3);
    };
    LGKM0_BAR();
    TSTAMP(1);
#pragma unroll
    for (int mf = 0; mf < MF; ++mf) rdA(0, mf);
#pragma unroll
    for (int nf = 0; nf < NF - 1; ++nf) { rdL(0, nf); rdH(0, nf); }
    LGKM0(); SB0();
    int slot = 0;
#pragma unroll 1
    for (int t = 0; t < NT - 1; ++t) {
      grp(0, 0, 0, NF - 1, 0);
#pragma unroll
      for (int nf = 1; nf < NF; ++nf) grp(0, nf, 1, nf - 1, nf);
      grp(1, 0, 1, NF - 1, -1);
      slot = (slot + 1 == NS) ? 0 : slot + 1;
      const int so = slot * STG;
      mm(1, 1, 0); pb[0] = vadd(b0[0], so); SB0(); mm(1, 1, 1); pb[1] = vadd(b0[1], so); SB0();
      mm(1, 1, 2); pa[0] = vadd(a0[0], so); SB0(); mm(1, 1, 3); pa[1] = vadd(a0[1], so); SB0();
      LGKM0_BAR(); SB0();
      mm(1, 2, 0); rdL(0, 0); mm(1, 2, 1); rdH(0, 0); mm(1, 2, 2); rdL(0, 1); mm(1, 2, 3); rdH(0, 1);
      mm(1, 3, 0); rdL(0, 2); mm(1, 3, 1); rdH(0, 2); mm(1, 3, 2); rdA(0, 0); mm(1, 3, 3); rdA(0, 1);
      mm(1, 4, 0); rdL(0, 3); mm(1, 4, 1); rdH(0, 3); mm(1, 4, 2); rdA(0, 2); mm(1, 4, 3); rdA(0, 3);
      grp(1, 5, 0, 4, -1);
      grp(1, 6, 0, 5, -1);
    }
    grp(0, 0, 0, NF - 1, 0);
#pragma unroll
    for (int nf = 1; nf < NF; ++nf) grp(0, nf, 1, nf - 1, nf);
    grp(1, 0, 1, NF - 1, -1);
#pragma unroll
    for (int nf = 1; nf < NF; ++nf) grp(1, nf, 0, -1, -1);
  }
  __builtin_amdgcn_s_setprio(0);
  TSTAMP(2);

  auto rsC = __builtin_amdgcn_make_buffer_rsrc((void*)C, (short)0, S_ * LDC * 2, 0x00020000);
  const int cofs = (g & 1) * 16 + (g >> 1) * 8;
  if constexpr (KSPLIT) {
    lds_ptr xb = smem + XTRA + cw * 8192;
#pragma unroll
    for (int mf = 0; mf < MF; ++mf)
#pragma unroll
      for (int j = 0; j < 2; ++j) *(LDS_AS f32x4*)(xb + (mf * 2 + j) * 1024 + lane * 16) = acc[mf][2 + j];
    LDS_AS float* st = (LDS_AS float*)(smem + XTRA + 32768);
    if (MODE == 1) {
#pragma unroll
      for (int mf = 0; mf < MF; ++mf) {
        float d = (fr & 3) == 0 ? sq[mf][0] : (fr & 3) == 1 ? sq[mf][1] : (fr & 3) == 2 ? sq[mf][2] : sq[mf][3];
        if (g == (fr >> 2)) *(LDS_AS f32x2*)(st + (wx * 128 + wm * 64 + mf * 16 + fr) * 2) = f32x2{rs[mf][0], d};
      }
      if (fr == 0) {
        *(LDS_AS f32x4*)(st + 512 + wx * 64 + keep * 32 + g * 4) = cs[0];
        *(LDS_AS f32x4*)(st + 512 + wx * 64 + keep * 32 + 16 + g * 4) = cs[1];
      }
    }
    LGKM0_BAR();
    lds_ptr pbuf = smem + XTRA + (cw ^ 2) * 8192;
    f32x4 v[MF][2];
#pragma unroll
    for (int mf = 0; mf < MF; ++mf)
#pragma unroll
      for (int j = 0; j < 2; ++j) v[mf][j] = acc[mf][j] + *(LDS_AS f32x4*)(pbuf + (mf * 2 + j) * 1024 + lane * 16);
    if (MODE == 1) {
      f32x4 c[2];
#pragma unroll
      for (int j = 0; j < 2; ++j)
        c[j] = *(LDS_AS f32x4*)(st + 512 + keep * 32 + j * 16 + g * 4) + *(LDS_AS f32x4*)(st + 512 + 64 + keep * 32 + j * 16 + g * 4);
      const int grp = col0 >> 8, dl = (grp == 0) ? 1 : (grp == 1) ? -1 : (grp == 2) ? 2 : -2;
#pragma unroll
      for (int mf = 0; mf < MF; ++mf) {
        int r = wm * 64 + mf * 16 + fr;
        f32x2 a = *(LDS_AS f32x2*)(st + r * 2) + *(LDS_AS f32x2*)(st + (128 + r) * 2);
        float m = a[0] * (1.0f / D_), var = a[1] * (1.0f / D_) - m * m;
        float rsd = __builtin_amdgcn_rsqf(fmaxf(var, 0.f) + 1e-5f), mr = -m * rsd;
        unsigned pk[2][2];
#pragma unroll
        for (int j = 0; j < 2; ++j) {
          f32x4 y = v[mf][j] * rsd + c[j] * mr;
          pk[j][0] = gelu2_pack(f32x2{y[0], y[1]}); pk[j][1] = gelu2_pack(f32x2{y[2], y[3]});
        }
        auto s0 = __builtin_amdgcn_permlane16_swap(pk[0][0], pk[1][0], false, false);
        auto s1 = __builtin_amdgcn_permlane16_swap(pk[0][1], pk[1][1], false, false);
        u32x4 o = {s0[0], s1[0], s0[1], s1[1]};
        int dr = row0 + r + dl;
        if (dr < 0 || dr >= S_) { o = u32x4{0u, 0u, 0u, 0u}; dr &= (S_ - 1); }
        __builtin_amdgcn_raw_buffer_store_b128(o, rsC, (dr * LDC + col0 + keep * 32 + cofs) * 2, 0, STAUX);
      }
    } else {
#pragma unroll
      for (int mf = 0; mf < MF; ++mf) {
        float y[8];
#pragma unroll
        for (int e = 0; e < 4; ++e) {
          float f0 = v[mf][0][e], f1 = v[mf][1][e];
          auto s = __builtin_amdgcn_permlane16_swap(__float_as_uint(f0), __float_as_uint(f1), false, false);
          unsigned u0 = s[0], u1 = s[1];
          y[e] = __uint_as_float(u0); y[4 + e] = __uint_as_float(u1);
        }
        u32x4 o;
#pragma unroll
        for (int e = 0; e < 4; ++e) o[e] = pack_bf16(y[2 * e] + bf_lo(res[mf][e]), y[2 * e + 1] + bf_hi(res[mf][e]));
        const int r = row0 + wm * 64 + mf * 16 + fr;
        __builtin_amdgcn_raw_buffer_store_b128(o, rsC, (r * LDC + col0 + keep * 32 + cofs) * 2, 0, MODE == 4 ? 0 : STAUX);
        if (MODE == 2) {
          float s = 0.f, qq = 0.f;
#pragma unroll
          for (int e = 0; e < 4; ++e) { float a = bf_lo(o[e]), b = bf_hi(o[e]); s += a + b; qq += a * a + b * b; }
          s = xsum32(xsum16(s)); qq = xsum32(xsum16(qq));
          if (g == 0) *(f32x2*)(stats + (size_t)r * 64 + (bn * 2 + keep) * 2) = f32x2{s, qq};
        }
      }
    }
  } else {
    LGKM0_BAR();
    f32x4 c[NF];
#pragma unroll
    for (int nf = 0; nf < NF; ++nf) c[nf] = *(LDS_AS f32x4*)(smem + XTRA + ((wx * NF + nf) * 16 + g * 4) * 4);
#pragma unroll
    for (int mf = 0; mf < MF; ++mf) {
      const f32x2 sr = *(LDS_AS f32x2*)(smem + XTRA + 1024 + (wm * 64 + mf * 16 + fr) * 8);
      const int co = ((row0 + wm * 64 + mf * 16 + fr) * LDC + col0 + wx * (NF * 16)) * 2;
      unsigned pk[NF][2];
#pragma unroll
      for (int nf = 0; nf < NF; ++nf) {
        f32x4 y = acc[mf][nf] * sr[0] + c[nf] * sr[1];
        pk[nf][0] = gelu2_pack(f32x2{y[0], y[1]}); pk[nf][1] = gelu2_pack(f32x2{y[2], y[3]});
        if (nf & 1) {
          auto s0 = __builtin_amdgcn_permlane16_swap(pk[nf - 1][0], pk[nf][0], false, false);
          auto s1 = __builtin_amdgcn_permlane16_swap(pk[nf - 1][1], pk[nf][1], false, false);
          __builtin_amdgcn_raw_buffer_store_b128(u32x4{s0[0], s1[0], s0[1], s1[1]}, rsC, co + ((nf - 1) * 16 + cofs) * 2, 0, STAUX);
        }
      }
      typedef __attribute__((ext_vector_type(2))) unsigned u32x2;
      __builtin_amdgcn_raw_buffer_store_b64(u32x2{pk[NF - 1][0], pk[NF - 1][1]}, rsC, co + ((NF - 1) * 16 + g * 4) * 2, 0, STAUX);
    }
  }
  TSTAMP(3);
#endif
}

template <int MODE> constexpr int lds_bytes() {
  return Cfg<MODE>::NS * (BM * BK * 2 + BK * Cfg<MODE>::BN * 2) + (MODE == 3 ? 2048 : MODE == 4 ? 32768 : 40960 + 16384);
}

extern "C" void kernel_launch(void* const* d_in, const int* in_sizes, int n_in, void* d_out, int out_size, void* d_ws,
                              size_t ws_size, hipStream_t stream) {
  const u16* x = (const u16*)d_in[0];
  const u16* w1 = (const u16*)d_in[1];
  const u16* w2 = (const u16*)d_in[2];
  const u16* w3 = (const u16*)d_in[3];
  const u16* w4 = (const u16*)d_in[4];
  char* ws = (char*)d_ws;
  u16* hs = (u16*)(ws + WS_HS);
  u16* x1 = (u16*)(ws + WS_X1);
  u16* h2 = (u16*)(ws + WS_H2);
  float* st = (float*)(ws + WS_ST);
  float* cs = (float*)(ws + WS_CS);
  (void)hipFuncSetAttribute((const void*)gemm_k<1>, hipFuncAttributeMaxDynamicSharedMemorySize, lds_bytes<1>());
  (void)hipFuncSetAttribute((const void*)gemm_k<2>, hipFuncAttributeMaxDynamicSharedMemorySize, lds_bytes<2>());
  (void)hipFuncSetAttribute((const void*)gemm_k<3>, hipFuncAttributeMaxDynamicSharedMemorySize, lds_bytes<3>());
  (void)hipFuncSetAttribute((const void*)gemm_k<4>, hipFuncAttributeMaxDynamicSharedMemorySize, lds_bytes<4>());
  gemm_k<1><<<256, 512, lds_bytes<1>(), stream>>>(x, w1, hs, x, st, cs, w3, w2);
  gemm_k<2><<<256, 512, lds_bytes<2>(), stream>>>(hs, w2, x1, x, st, cs, w3, w2);
  gemm_k<3><<<256, 512, lds_bytes<3>(), stream>>>(x1, w3, h2, x, st, cs, w3, w4);
  gemm_k<4><<<256, 512, lds_bytes<4>(), stream>>>(h2, w4, (u16*)d_out, x1, st, cs, w3, w4);
}
```

```cpp
#include <hip/hip_runtime.h>
#include <hip/hip_bf16.h>
#include <stdint.h>
#include <type_traits>

typedef unsigned short u16;
typedef __attribute__((ext_vector_type(8))) short bf16x8;
typedef __attribute__((ext_vector_type(4))) short s16x4;
typedef __attribute__((ext_vector_type(4))) float f32x4;
typedef __attribute__((ext_vector_type(2))) float f32x2;
typedef __attribute__((ext_vector_type(4))) unsigned u32x4;
typedef __attribute__((ext_vector_type(2))) __bf16 bf16x2_t;
#define LDS_AS __attribute__((address_space(3)))
typedef LDS_AS char* lds_ptr;

constexpr int S_ = 2048, D_ = 1024, F_ = 3584;
constexpr int BK = 64;

constexpr size_t WS_HS = 0;
constexpr size_t WS_X1 = WS_HS + (size_t)S_ * D_ * 2;
constexpr size_t WS_H2 = WS_X1 + (size_t)S_ * D_ * 2;
constexpr size_t WS_ST = WS_H2 + (size_t)S_ * F_ * 2;
constexpr size_t WS_CS = WS_ST + (size_t)S_ * 32 * 2 * 4;
constexpr size_t WS_FL = WS_CS + (size_t)16 * F_ * 4;
constexpr size_t WS_EX = WS_FL + (size_t)1024 * 128;

__device__ __forceinline__ unsigned pack_bf16(float a, float b) {
  f32x2 v = {a, b};
  bf16x2_t r = __builtin_convertvector(v, bf16x2_t);
  return __builtin_bit_cast(unsigned, r);
}
__device__ __forceinline__ float bf_lo(unsigned u) { return __builtin_bit_cast(float, u << 16); }
__device__ __forceinline__ float bf_hi(unsigned u) { return __builtin_bit_cast(float, u & 0xffff0000u); }

__device__ __forceinline__ unsigned gelu2s_pack(f32x2 u, float K1, float K0, float ri) {
  f32x2 z = u * (u * u * K1 + K0);
  f32x2 e = {__builtin_amdgcn_exp2f(z[0]), __builtin_amdgcn_exp2f(z[1])};
  e = e * ri + ri;
  f32x2 r = {__builtin_amdgcn_rcpf(e[0]), __builtin_amdgcn_rcpf(e[1])};
  u *= r;
  return pack_bf16(u[0], u[1]);
}
constexpr float GK0 = -2.0f * 0.7978845608028654f * 1.4426950408889634f, GK1 = GK0 * 0.044715f;
__device__ __forceinline__ float xsum16(float v) {
  auto s = __builtin_amdgcn_permlane16_swap(__float_as_uint(v), __float_as_uint(v), false, false);
  unsigned a = s[0], b = s[1];
  return __uint_as_float(a) + __uint_as_float(b);
}
__device__ __forceinline__ float xsum32(float v) {
  auto s = __builtin_amdgcn_permlane32_swap(__float_as_uint(v), __float_as_uint(v), false, false);
  unsigned a = s[0], b = s[1];
  return __uint_as_float(a) + __uint_as_float(b);
}
#define SB0() __builtin_amdgcn_sched_barrier(0)
template <int N> __device__ __forceinline__ void wait_vm() { asm volatile("s_waitcnt vmcnt(%0)" ::"i"(N) : "memory"); }
__device__ __forceinline__ int vadd(int a, int s) { int r; asm volatile("v_add_u32 %0, %1, %2" : "=v"(r) : "s"(s), "v"(a)); return r; }
#define STAUX 0
#define STAUX3 16
#define WAIT_VM(n) asm volatile("s_waitcnt vmcnt(%0)" ::"i"(n) : "memory")
#define BAR() asm volatile("s_barrier" ::: "memory")
#define LGKM0() __builtin_amdgcn_s_waitcnt(0xC07F)
#define LGKM0_BAR() do { LGKM0(); asm volatile("s_barrier" ::: "memory"); } while (0)

template <int MODE> struct Cfg;
template <> struct Cfg<1> { static constexpr int BN = 64,  K = 1024, LDA = 1024, LDB = 1024, LDC = 1024, NS = 4; };
template <> struct Cfg<2> { static constexpr int BN = 64,  K = 1024, LDA = 1024, LDB = 1024, LDC = 1024, NS = 4; };
template <> struct Cfg<3> { static constexpr int BN = 224, K = 1024, LDA = 1024, LDB = 3584, LDC = 3584, NS = 3; };
template <> struct Cfg<4> { static constexpr int BN = 128, K = 1792, LDA = 3584, LDB = 1024, LDC = 1024, NS = 4; };
constexpr int BM = 128;
constexpr int cmin(int a, int b) { return a < b ? a : b; }
constexpr int ramp(int t, int NS, int NT) { return t == 0 ? 2 : cmin(cmin(ramp(t - 1, NS, NT) + 2, t - 1 + NS), NT); }

template <int MODE>
__global__ __launch_bounds__(512, 2) void gemm_k(const u16* __restrict__ A, const u16* __restrict__ B, u16* __restrict__ C,
                                                 const u16* __restrict__ R, float* __restrict__ stats,
                                                 float* __restrict__ csum, const u16* __restrict__ P0, const u16* __restrict__ P1) {
#if defined(__HIP_DEVICE_COMPILE__)
  using G = Cfg<MODE>;
  constexpr int BN = G::BN, K = G::K, LDA = G::LDA, LDB = G::LDB, LDC = G::LDC, NS = G::NS;
  constexpr bool KSPLIT = (MODE <= 2);
  constexpr int NT = K / BK;
  constexpr int A_BYTES = BM * BK * 2, B_BYTES = BK * BN * 2, STG = A_BYTES + B_BYTES;
  constexpr int CPR = BN / 8;
  constexpr int LA = 4, LB = BN / 32, L = LA + LB;
  constexpr int NF = MODE == 3 ? 7 : 4, MF = 4;
  extern __shared__ __attribute__((aligned(16))) char smem_raw[];
  lds_ptr smem = (lds_ptr)smem_raw;
  constexpr int XTRA = NS * STG;

  const int tid = threadIdx.x, lane = tid & 63;
  const int wave = __builtin_amdgcn_readfirstlane(tid >> 6);
  const int xcd = blockIdx.x & 7, bi = blockIdx.x >> 3;
  const int kh = (MODE == 4) ? (xcd & 1) : 0;
  constexpr int PX = 1;
  const int bm = (MODE == 3) ? (xcd >> 2) * 8 + (bi & 7) : (xcd >> 1) * 4 + (bi & 3);
  const int bn = (MODE == 3) ? (xcd & 3) * 4 + (bi >> 3) : (MODE == 4) ? (bi >> 2) : (xcd & 1) * 8 + (bi >> 2);
  const int row0 = bm * BM, col0 = bn * BN;

  if (__builtin_expect(wave < 4, 1)) {
    const int w = wave;

    const int t0 = (((xcd * NT) >> 3)) % NT;
    auto rsA = __builtin_amdgcn_make_buffer_rsrc((void*)(A + (size_t)row0 * LDA + kh * (K * BM)), (short)0, BM * K * 2, 0x00020000);
    auto rsB = __builtin_amdgcn_make_buffer_rsrc((void*)(B + (size_t)kh * K * LDB + col0), (short)0, (K * LDB - col0) * 2, 0x00020000);
    const int rA = w * 8 + (lane >> 3);
    const int voA = (MODE == 4) ? w * 1024 + (lane & 31) * 32 + (lane >> 5) * 16 : rA * LDA * 2 + (((lane & 7) ^ ((rA >> 1) & 7)) << 4);
    constexpr int AST = (MODE == 4) ? 16384 : BK * 2, ASI = (MODE == 4) ? 4096 : 32 * LDA * 2;
    int voB[LB];
    auto issueA = [&](int t, int slot) {
      t += t0; if (t >= NT) t -= NT;
#pragma unroll
      for (int i = 0; i < LA; ++i)
        __builtin_amdgcn_raw_ptr_buffer_load_lds(rsA, (LDS_AS void*)(smem + slot * STG + (w + 4 * i) * 1024), 16, voA, t * AST + i * ASI, 0, 0);
    };
    auto issueB = [&](int t, int slot) {
      t += t0; if (t >= NT) t -= NT;
#pragma unroll
      for (int i = 0; i < LB; ++i)
        __builtin_amdgcn_raw_ptr_buffer_load_lds(rsB, (LDS_AS void*)(smem + slot * STG + A_BYTES + (w + 4 * i) * 1024), 16, voB[i], t * (BK * LDB * 2), 0, 0);
    };
    issueA(0, 0);
    SB0();
#pragma unroll
    for (int i = 0; i < LB; ++i) {
      int p = (w + 4 * i) * 64 + lane, r = p / CPR, pc = p % CPR;
      int sw = (BN == 64) ? ((((r >> 1) & 1) | (((r >> 3) & 1) << 1)) << 1) : (BN == 128) ? (((r & 3) | (((r >> 3) & 1) << 2)) << 1) : (((r >> 3) & 1) << 1);
      voB[i] = r * LDB * 2 + ((pc ^ sw) << 4);
    }
    issueB(0, 0);
    issueA(1, 1); issueB(1, 1);
    wait_vm<L>();
    BAR();
    auto rampstep = [&](auto tc) {
      constexpr int t = decltype(tc)::value, s0 = ramp(t - 1, NS, NT), s1 = ramp(t, NS, NT);
#pragma unroll
      for (int s = s0; s < s1; ++s) { issueA(s, s % NS); issueB(s, s % NS); }
      wait_vm<L * (s1 - (t + 1))>();
      BAR();
    };
    rampstep(std::integral_constant<int, 1>{});
    rampstep(std::integral_constant<int, 2>{});
    static_assert(ramp(2, NS, NT) == 1 + NS, "ramp");
    int slot = (1 + NS) % NS;
    for (int t = 3; t <= NT - NS + 1; ++t) {
      issueA(t - 2 + NS, slot); issueB(t - 2 + NS, slot);
      slot = (slot + 1 == NS) ? 0 : slot + 1;
      wait_vm<L * (NS - 2)>();
      BAR();
    }
    f32x4 sv[8]; float cv[16];
    constexpr int XL = (MODE == 3) ? 24 : (MODE == 4) ? 0 : 4;
    const int sjb = blockIdx.x < 224 ? blockIdx.x : 0;
    if (MODE == 1 || MODE == 2) {
      auto rsW = __builtin_amdgcn_make_buffer_rsrc((void*)(P0 + (size_t)((sjb / 28) * 128 + (MODE - 1) * 64) * F_ + (sjb % 28) * 128), (short)0, (63 * F_ + 128) * 2, 0x00020000);
      int vo = (lane >> 2) * F_ * 2 + w * 64 + (lane & 3) * 16;
#pragma unroll
      for (int i = 0; i < 4; ++i)
        __builtin_amdgcn_raw_ptr_buffer_load_lds(rsW, (LDS_AS void*)(smem + XTRA + 40960 + (w * 4 + i) * 1024), 16, vo, i * 16 * F_ * 2, 0, 0);
    }
    if (MODE == 3) {
      const f32x4* p = (const f32x4*)(stats + (size_t)(row0 + (tid >> 1)) * 64 + (tid & 1) * 32);
#pragma unroll
      for (int i = 0; i < 8; ++i) sv[i] = p[i];
      const int c = col0 + (tid < BN ? tid : 0);
#pragma unroll
      for (int i = 0; i < 16; ++i) cv[i] = csum[i * F_ + c];
    }
    if (NS == 4) { wait_vm<L + XL>(); BAR(); }
    wait_vm<XL>();
    BAR();
    if (KSPLIT) BAR();
    unsigned pfd = 0;
    if (MODE == 1 || MODE == 2) {
      if (MODE == 1 && w == 0) asm volatile("global_load_dword %0, %1, off" : "+v"(pfd) : "v"((const char*)P1 + (size_t)(blockIdx.x * 64 + lane) * 128) : "memory");
      if (MODE == 1 && tid < 4) *(unsigned*)((char*)csum + (WS_FL - WS_CS) + (blockIdx.x * 4 + tid) * 128) = 0u;
      wait_vm<0>();
      float s0 = 0.f, s1 = 0.f;
#pragma unroll
      for (int r = 0; r < 16; ++r) {
        unsigned u = *(LDS_AS unsigned*)(smem + XTRA + 40960 + w * 4096 + r * 256 + lane * 4);
        s0 += bf_lo(u); s1 += bf_hi(u);
      }
      s0 = xsum32(xsum16(s0)); s1 = xsum32(xsum16(s1));
      if (blockIdx.x < 224 && lane < 16)
        *(f32x2*)(csum + (size_t)((sjb / 28) * 2 + (MODE - 1)) * F_ + (sjb % 28) * 128 + w * 32 + lane * 2) = f32x2{s0, s1};
    }
    if (MODE == 3) {
      f32x4 a = (sv[0] + sv[1]) + (sv[2] + sv[3]) + ((sv[4] + sv[5]) + (sv[6] + sv[7]));
      float s = a[0] + a[2], q = a[1] + a[3];
      s += __shfl_xor(s, 1); q += __shfl_xor(q, 1);
      float m = s * (1.0f / D_), v = q * (1.0f / D_) - m * m;
      const float ve = fmaxf(v, 0.f) + 1e-5f, rsd = __builtin_amdgcn_rsqf(ve);
      if ((tid & 1) == 0) *(LDS_AS f32x4*)(smem + XTRA + 1024 + (tid >> 1) * 16) = f32x4{-m, GK1 * rsd * rsd * rsd, GK0 * rsd, ve * rsd};
      float c = 0.f;
#pragma unroll
      for (int i = 0; i < 16; ++i) c += cv[i];
      *(LDS_AS float*)(smem + XTRA + tid * 4) = c;
      if (tid < 224) asm volatile("global_load_dword %0, %1, off" : "+v"(pfd) : "v"((const char*)P1 + (size_t)(blockIdx.x * 224 + tid) * 128) : "memory");
      LGKM0_BAR();
    }
    asm volatile("s_waitcnt vmcnt(0)" : "+v"(pfd) :: "memory");
    return;
  }

  const int cw = wave - 4;
  const int wm = cw & 1, wx = cw >> 1;
  const int g = lane >> 4, fr = lane & 15;
  const int keep = wx ^ wm;
  __builtin_amdgcn_s_setprio(1);
  unsigned pfc = 0;
  u32x4 res[MF];
  if (MODE == 4) {
#pragma unroll
    for (int i = 0; i < 4; ++i)
      res[i] = *(const u32x4*)(R + (size_t)(row0 + wm * 64 + kh * 32 + (i >> 1) * 16 + fr) * 1024 + col0 + wx * 64 + (i & 1) * 32 + (g & 1) * 16 + (g >> 1) * 8);
  }
  if (MODE == 2) {
#pragma unroll
    for (int mf = 0; mf < MF; ++mf)
      res[mf] = *(const u32x4*)(R + (size_t)(row0 + wm * 64 + mf * 16 + fr) * 1024 + col0 + keep * 32 + (g & 1) * 16 + (g >> 1) * 8);
  }
  f32x4 acc[MF][NF];
#pragma unroll
  for (int i = 0; i < MF; ++i)
#pragma unroll
    for (int j = 0; j < NF; ++j) acc[i][j] = f32x4{0.f, 0.f, 0.f, 0.f};
  f32x4 rs[MF], sq[MF], cs[2];
#pragma unroll
  for (int i = 0; i < MF; ++i) { rs[i] = f32x4{0.f, 0.f, 0.f, 0.f}; sq[i] = f32x4{0.f, 0.f, 0.f, 0.f}; }
  cs[0] = cs[1] = f32x4{0.f, 0.f, 0.f, 0.f};
  const bf16x8 ones = {0x3f80, 0x3f80, 0x3f80, 0x3f80, 0x3f80, 0x3f80, 0x3f80, 0x3f80};
  const int sbase = (int)(uintptr_t)smem;
  const int q = fr >> 2, p = fr & 3;
  auto ldA = [&](int addr, int off) { return *(LDS_AS bf16x8*)(uintptr_t)(addr + off); };
  auto ldT = [&](int addr, int off) { return __builtin_amdgcn_ds_read_tr16_b64_v4i16((LDS_AS s16x4*)(uintptr_t)(addr + off)); };
#define CAT(l, h) __builtin_shufflevector(l, h, 0, 1, 2, 3, 4, 5, 6, 7)
#define MFMA(a, b, c) __builtin_amdgcn_mfma_f32_16x16x32_bf16(a, b, c, 0, 0, 0)

  if constexpr (KSPLIT) {
    const int a0 = sbase + (wm * 64 + fr) * 128 + (((wx * 4 + g) ^ ((fr >> 1) & 7)) << 4);
    const int swB = ((q >> 1) & 1) | ((g & 1) << 1);
    int b0[4];
#pragma unroll
    for (int nf = 0; nf < 4; ++nf) b0[nf] = sbase + A_BYTES + (8 * g + q + wx * 32) * 128 + (((nf ^ (2 * keep)) ^ swB) << 5) + p * 8;
    bf16x8 xa[2][4]; s16x4 wl[2][4], wh[2][4];
    int pa = a0, pb[4] = {b0[0], b0[1], b0[2], b0[3]};
    int slot = 0;
    auto step = [&](int c, bool RD) {
      const int n = c ^ 1;
      int so = 0;
      if (RD) { slot = (slot + 1 == NS) ? 0 : slot + 1; so = slot * STG; }
#pragma unroll
      for (int i = 0; i < 16; ++i) {
        const int nf = i >> 2, mf = i & 3;
        acc[mf][nf] = MFMA(CAT(wl[c][nf], wh[c][nf]), xa[c][mf], acc[mf][nf]); SB0();
        if (RD) {
          if (i == 0) xa[n][0] = ldA(pa, 0);
          else if (i == 1) wl[n][0] = ldT(pb[0], 0);
          else if (i == 2) wh[n][0] = ldT(pb[0], 512);
          else if (i < 6) xa[n][i - 2] = ldA(pa, (i - 2) * 2048);
          else if (i < 12 && (i & 1)) wh[n][(i - 4) >> 1] = ldT(pb[(i - 4) >> 1], 512);
          else if (i < 12) wl[n][(i - 4) >> 1] = ldT(pb[(i - 4) >> 1], 0);
          else if (i == 12) { pa = vadd(a0, so); pb[0] = vadd(b0[0], so); }
          else if (i < 16) pb[i - 12] = vadd(b0[i - 12], so);
          SB0();
        }
        continue;
        if (RD) {
          if (i < 2) { xa[n][2 * i] = ldA(pa, 2 * i * 2048); xa[n][2 * i + 1] = ldA(pa, (2 * i + 1) * 2048); }
          else if (i < 6) { wl[n][i - 2] = ldT(pb[i - 2], 0); wh[n][i - 2] = ldT(pb[i - 2], 512); }
          else if (i == 6) pa = vadd(a0, so);
          else if (i < 11) pb[i - 7] = vadd(b0[i - 7], so);
          SB0();
        }
      }
      if (MODE == 1) {
#pragma unroll
        for (int mf = 0; mf < 4; ++mf) { rs[mf] = MFMA(ones, xa[c][mf], rs[mf]); sq[mf] = MFMA(xa[c][mf], xa[c][mf], sq[mf]); }
        cs[0] = MFMA(CAT(wl[c][0], wh[c][0]), ones, cs[0]);
        cs[1] = MFMA(CAT(wl[c][1], wh[c][1]), ones, cs[1]);
        SB0();
      }
    };
    LGKM0_BAR();
#pragma unroll
    for (int i = 0; i < 4; ++i) { xa[0][i] = ldA(pa, i * 2048); wl[0][i] = ldT(pb[i], 0); wh[0][i] = ldT(pb[i], 512); }
    slot = 1; pa = a0 + STG;
#pragma unroll
    for (int i = 0; i < 4; ++i) pb[i] = b0[i] + STG;
    SB0();
#pragma unroll 1
    for (int t = 0; t < NT - 2; t += 2) {
      LGKM0_BAR(); SB0(); step(0, true);
      LGKM0_BAR(); SB0(); step(1, true);
    }
    LGKM0_BAR(); SB0(); step(0, true);
    step(1, false);
  } else if constexpr (MODE == 4) {
    int a0[2], b0[4];
#pragma unroll
    for (int h = 0; h < 2; ++h) a0[h] = sbase + (g >> 1) * 4096 + (wm * 2 + (h ^ kh)) * 1024 + (g & 1) * 512 + fr * 16;
#pragma unroll
    for (int nf = 0; nf < 4; ++nf) b0[nf] = sbase + A_BYTES + (8 * g + q) * 256 + (((wx * 4 + nf) ^ (q | ((g & 1) << 2))) << 5) + p * 8;
    bf16x8 xa[2][4]; s16x4 wl[2][4], wh[2][4];
    int pa[2] = {a0[0], a0[1]}, pb[4] = {b0[0], b0[1], b0[2], b0[3]};
    int slot = 0;
    auto half = [&](int c, bool RD, bool ADV) {
      const int n = c ^ 1;
      int so = 0;
      if (ADV) { slot = (slot + 1 == NS) ? 0 : slot + 1; so = slot * STG; }
#pragma unroll
      for (int i = 0; i < 16; ++i) {
        const int nf = i >> 2, mf = i & 3;
        acc[mf][nf] = MFMA(CAT(wl[c][nf], wh[c][nf]), xa[c][mf], acc[mf][nf]); SB0();
        if (RD && i < 12) {
          if (i == 0) xa[n][0] = ldA(pa[0], n * 8192);
          else if (i == 1) wl[n][0] = ldT(pb[0], n * 8192);
          else if (i == 2) wh[n][0] = ldT(pb[0], n * 8192 + 1024);
          else if (i < 6) xa[n][i - 2] = ldA(pa[(i - 2) >> 1], n * 8192 + ((i - 2) & 1) * 256);
          else if (i & 1) wh[n][(i - 4) >> 1] = ldT(pb[(i - 4) >> 1], n * 8192 + 1024);
          else wl[n][(i - 4) >> 1] = ldT(pb[(i - 4) >> 1], n * 8192);
        }
        if (ADV) {
          if (i == 12) { pa[0] = vadd(a0[0], so); pb[0] = vadd(b0[0], so); }
          if (i == 13) { pa[1] = vadd(a0[1], so); pb[1] = vadd(b0[1], so); }
          if (i == 14) { pb[2] = vadd(b0[2], so); pb[3] = vadd(b0[3], so); }
        }
        SB0();
      }
    };
    LGKM0_BAR();
#pragma unroll
    for (int i = 0; i < 4; ++i) { xa[0][i] = ldA(pa[i >> 1], (i & 1) * 256); wl[0][i] = ldT(pb[i], 0); wh[0][i] = ldT(pb[i], 1024); }
    LGKM0(); SB0();
#pragma unroll 1
    for (int t = 0; t < NT - 1; ++t) {
      half(0, true, true);
      LGKM0_BAR(); SB0();
      half(1, true, false);
    }
    half(0, true, false);
    LGKM0(); SB0();
    half(1, false, false);
  } else {
    int a0[2], b0[2];
#pragma unroll
    for (int kk = 0; kk < 2; ++kk) a0[kk] = sbase + (wm * 64 + fr) * 128 + (((kk * 4 + g) ^ ((fr >> 1) & 7)) << 4);
#pragma unroll
    for (int e = 0; e < 2; ++e) b0[e] = sbase + A_BYTES + (8 * g + q) * (BN * 2) + ((((wx * NF + e) ^ (g & 1)) - e) << 5) + p * 8;
    bf16x8 xa[2][4]; s16x4 wl[NF], wh[NF];
    int pa[2] = {a0[0], a0[1]}, pb[2] = {b0[0], b0[1]};
    auto rdL = [&](int kk, int nf) { wl[nf] = ldT(pb[nf & 1], nf * 32 + kk * 32 * BN * 2); SB0(); };
    auto rdH = [&](int kk, int nf) { wh[nf] = ldT(pb[nf & 1], nf * 32 + kk * 32 * BN * 2 + 4 * BN * 2); SB0(); };
    auto rdA = [&](int kk, int mf) { xa[kk][mf] = ldA(pa[kk], mf * 2048); SB0(); };
    auto mm = [&](int kk, int nf, int mf) { acc[mf][nf] = MFMA(CAT(wl[nf], wh[nf]), xa[kk][mf], acc[mf][nf]); SB0(); };
    auto grp = [&](int kk, int nf, int pk, int pn, int amf) {
      mm(kk, nf, 0); if (pn >= 0) rdL(pk, pn);
      mm(kk, nf, 1); if (pn >= 0) rdH(pk, pn);
      mm(kk, nf, 2); if (amf >= 0 && amf < MF) rdA(kk ^ 1, amf);
      mm(kk, nf, 3);
    };
    LGKM0_BAR();
#pragma unroll
    for (int mf = 0; mf < MF; ++mf) rdA(0, mf);
#pragma unroll
    for (int nf = 0; nf < NF - 1; ++nf) { rdL(0, nf); rdH(0, nf); }
    LGKM0(); SB0();
    int slot = 0;
#pragma unroll 1
    for (int t = 0; t < NT - 1; ++t) {
      grp(0, 0, 0, NF - 1, 0);
#pragma unroll
      for (int nf = 1; nf < NF; ++nf) grp(0, nf, 1, nf - 1, nf);
      grp(1, 0, 1, NF - 1, -1);
      slot = (slot + 1 == NS) ? 0 : slot + 1;
      const int so = slot * STG;
      mm(1, 1, 0); pb[0] = vadd(b0[0], so); SB0(); mm(1, 1, 1); pb[1] = vadd(b0[1], so); SB0();
      mm(1, 1, 2); pa[0] = vadd(a0[0], so); SB0(); mm(1, 1, 3); pa[1] = vadd(a0[1], so); SB0();
      LGKM0_BAR(); SB0();
      mm(1, 2, 0); rdL(0, 0); mm(1, 2, 1); rdH(0, 0); mm(1, 2, 2); rdL(0, 1); mm(1, 2, 3); rdH(0, 1);
      mm(1, 3, 0); rdL(0, 2); mm(1, 3, 1); rdH(0, 2); mm(1, 3, 2); rdA(0, 0); mm(1, 3, 3); rdA(0, 1);
      mm(1, 4, 0); rdL(0, 3); mm(1, 4, 1); rdH(0, 3); mm(1, 4, 2); rdA(0, 2); mm(1, 4, 3); rdA(0, 3);
      grp(1, 5, 0, 4, -1);
      grp(1, 6, 0, 5, -1);
    }
    grp(0, 0, 0, NF - 1, 0);
#pragma unroll
    for (int nf = 1; nf < NF; ++nf) grp(0, nf, 1, nf - 1, nf);
    grp(1, 0, 1, NF - 1, -1);
#pragma unroll
    for (int nf = 1; nf < NF; ++nf) grp(1, nf, 0, -1, -1);
  }
  __builtin_amdgcn_s_setprio(0);

  auto rsC = __builtin_amdgcn_make_buffer_rsrc((void*)C, (short)0, S_ * LDC * 2, 0x00020000);
  const int cofs = (g & 1) * 16 + (g >> 1) * 8;
  if constexpr (KSPLIT) {
    lds_ptr xb = smem + XTRA + cw * 8192;
#pragma unroll
    for (int mf = 0; mf < MF; ++mf)
#pragma unroll
      for (int j = 0; j < 2; ++j) *(LDS_AS f32x4*)(xb + (mf * 2 + j) * 1024 + lane * 16) = acc[mf][2 + j];
    LDS_AS float* st = (LDS_AS float*)(smem + XTRA + 32768);
    if (MODE == 1) {
#pragma unroll
      for (int mf = 0; mf < MF; ++mf) {
        float d = (fr & 3) == 0 ? sq[mf][0] : (fr & 3) == 1 ? sq[mf][1] : (fr & 3) == 2 ? sq[mf][2] : sq[mf][3];
        if (g == (fr >> 2)) *(LDS_AS f32x2*)(st + (wx * 128 + wm * 64 + mf * 16 + fr) * 2) = f32x2{rs[mf][0], d};
      }
      if (fr == 0) {
        *(LDS_AS f32x4*)(st + 512 + wx * 64 + keep * 32 + g * 4) = cs[0];
        *(LDS_AS f32x4*)(st + 512 + wx * 64 + keep * 32 + 16 + g * 4) = cs[1];
      }
    }
    LGKM0_BAR();
    lds_ptr pbuf = smem + XTRA + (cw ^ 2) * 8192;
    f32x4 v[MF][2];
#pragma unroll
    for (int mf = 0; mf < MF; ++mf)
#pragma unroll
      for (int j = 0; j < 2; ++j) v[mf][j] = acc[mf][j] + *(LDS_AS f32x4*)(pbuf + (mf * 2 + j) * 1024 + lane * 16);
    if (MODE == 1) {
      f32x4 c[2];
#pragma unroll
      for (int j = 0; j < 2; ++j)
        c[j] = *(LDS_AS f32x4*)(st + 512 + keep * 32 + j * 16 + g * 4) + *(LDS_AS f32x4*)(st + 512 + 64 + keep * 32 + j * 16 + g * 4);
      const int grp = col0 >> 8, dl = (grp == 0) ? 1 : (grp == 1) ? -1 : (grp == 2) ? 2 : -2;
#pragma unroll
      for (int mf = 0; mf < MF; ++mf) {
        int r = wm * 64 + mf * 16 + fr;
        f32x2 a = *(LDS_AS f32x2*)(st + r * 2) + *(LDS_AS f32x2*)(st + (128 + r) * 2);
        float m = a[0] * (1.0f / D_), var = a[1] * (1.0f / D_) - m * m;
        const float ve = fmaxf(var, 0.f) + 1e-5f, rsd = __builtin_amdgcn_rsqf(ve), K1 = GK1 * rsd * rsd * rsd, K0 = GK0 * rsd, ri = ve * rsd;
        unsigned pk[2][2];
#pragma unroll
        for (int j = 0; j < 2; ++j) {
          f32x4 y = v[mf][j] - c[j] * m;
          pk[j][0] = gelu2s_pack(f32x2{y[0], y[1]}, K1, K0, ri); pk[j][1] = gelu2s_pack(f32x2{y[2], y[3]}, K1, K0, ri);
        }
        auto s0 = __builtin_amdgcn_permlane16_swap(pk[0][0], pk[1][0], false, false);
        auto s1 = __builtin_amdgcn_permlane16_swap(pk[0][1], pk[1][1], false, false);
        u32x4 o = {s0[0], s1[0], s0[1], s1[1]};
        int dr = row0 + r + dl;
        if (dr < 0 || dr >= S_) { o = u32x4{0u, 0u, 0u, 0u}; dr &= (S_ - 1); }
        __builtin_amdgcn_raw_buffer_store_b128(o, rsC, (dr * LDC + col0 + keep * 32 + cofs) * 2, 0, STAUX);
      }
    } else {
#pragma unroll
      for (int mf = 0; mf < MF; ++mf) {
        float y[8];
#pragma unroll
        for (int e = 0; e < 4; ++e) {
          float f0 = v[mf][0][e], f1 = v[mf][1][e];
          auto s = __builtin_amdgcn_permlane16_swap(__float_as_uint(f0), __float_as_uint(f1), false, false);
          unsigned u0 = s[0], u1 = s[1];
          y[e] = __uint_as_float(u0); y[4 + e] = __uint_as_float(u1);
        }
        u32x4 o;
#pragma unroll
        for (int e = 0; e < 4; ++e) o[e] = pack_bf16(y[2 * e] + bf_lo(res[mf][e]), y[2 * e + 1] + bf_hi(res[mf][e]));
        const int r = row0 + wm * 64 + mf * 16 + fr;
        __builtin_amdgcn_raw_buffer_store_b128(o, rsC, (r * LDC + col0 + keep * 32 + cofs) * 2, 0, STAUX);
        if (MODE == 2) {
          float s = 0.f, qq = 0.f;
#pragma unroll
          for (int e = 0; e < 4; ++e) { float a = bf_lo(o[e]), b = bf_hi(o[e]); s += a + b; qq += a * a + b * b; }
          s = xsum32(xsum16(s)); qq = xsum32(xsum16(qq));
          if (g == 0) *(f32x2*)(stats + (size_t)r * 64 + (bn * 2 + keep) * 2) = f32x2{s, qq};
        }
      }
    }
  } else if constexpr (MODE == 4) {
    typedef __attribute__((address_space(1))) unsigned gu32;
    char* wsb = (char*)csum - WS_CS;
    auto rsX = __builtin_amdgcn_make_buffer_rsrc((void*)(wsb + WS_EX), (short)0, 256 * 32768, 0x00020000);
    const int me = blockIdx.x * 4 + cw, other = (blockIdx.x ^ PX) * 4 + cw;
#pragma unroll
    for (int i = 0; i < 4; ++i) {
      const f32x4 p0 = acc[2 + (i >> 1)][(i & 1) * 2], p1 = acc[2 + (i >> 1)][(i & 1) * 2 + 1];
      __builtin_amdgcn_raw_buffer_store_b128(u32x4{pack_bf16(p0[0], p0[1]), pack_bf16(p0[2], p0[3]), pack_bf16(p1[0], p1[1]), pack_bf16(p1[2], p1[3])}, rsX, me * 4096 + lane * 16 + i * 1024, 0, 16);
    }
    wait_vm<0>();
    if (lane == 0) __hip_atomic_store((gu32*)(wsb + WS_FL + me * 128), 1u, __ATOMIC_RELAXED, __HIP_MEMORY_SCOPE_AGENT);
    for (int it = 0; it < (1 << 22); ++it) {
      unsigned f = __hip_atomic_load((gu32*)(wsb + WS_FL + other * 128), __ATOMIC_RELAXED, __HIP_MEMORY_SCOPE_AGENT);
      if (__builtin_amdgcn_readfirstlane(f) == 1u) break;
      __builtin_amdgcn_s_sleep(1);
    }
    asm volatile("" ::: "memory");
    u32x4 pv[4];
#pragma unroll
    for (int i = 0; i < 4; ++i) pv[i] = __builtin_amdgcn_raw_buffer_load_b128(rsX, lane * 16 + i * 1024, other * 4096, 16);
#pragma unroll
    for (int i = 0; i < 4; ++i) {
      const int j = i >> 1, n0 = (i & 1) * 2;
      f32x4 v0 = acc[j][n0] + f32x4{bf_lo(pv[i][0]), bf_hi(pv[i][0]), bf_lo(pv[i][1]), bf_hi(pv[i][1])};
      f32x4 v1 = acc[j][n0 + 1] + f32x4{bf_lo(pv[i][2]), bf_hi(pv[i][2]), bf_lo(pv[i][3]), bf_hi(pv[i][3])};
      float y[8];
#pragma unroll
      for (int e = 0; e < 4; ++e) {
        float f0 = v0[e], f1 = v1[e];
        auto s = __builtin_amdgcn_permlane16_swap(__float_as_uint(f0), __float_as_uint(f1), false, false);
        unsigned u0 = s[0], u1 = s[1];
        y[e] = __uint_as_float(u0); y[4 + e] = __uint_as_float(u1);
      }
      u32x4 o;
#pragma unroll
      for (int e = 0; e < 4; ++e) o[e] = pack_bf16(y[2 * e] + bf_lo(res[i][e]), y[2 * e + 1] + bf_hi(res[i][e]));
      __builtin_amdgcn_raw_buffer_store_b128(o, rsC, ((row0 + wm * 64 + kh * 32 + j * 16 + fr) * LDC + col0 + wx * 64 + n0 * 16 + cofs) * 2, 0, 0);
    }
  } else {
    LGKM0_BAR();
    f32x4 c[NF];
#pragma unroll
    for (int nf = 0; nf < NF; ++nf) c[nf] = *(LDS_AS f32x4*)(smem + XTRA + ((wx * NF + nf) * 16 + g * 4) * 4);
#pragma unroll
    for (int mf = 0; mf < MF; ++mf) {
      const f32x4 sr = *(LDS_AS f32x4*)(smem + XTRA + 1024 + (wm * 64 + mf * 16 + fr) * 16);
      const int co = (bm * 224 + bn * 14 + wx * NF) * 4096 + (wm * 64 + mf * 16 + fr) * 32;
      unsigned pk[NF][2];
#pragma unroll
      for (int nf = 0; nf < NF; ++nf) {
        f32x4 y = c[nf] * sr[0] + acc[mf][nf];
        pk[nf][0] = gelu2s_pack(f32x2{y[0], y[1]}, sr[1], sr[2], sr[3]); pk[nf][1] = gelu2s_pack(f32x2{y[2], y[3]}, sr[1], sr[2], sr[3]);
        if (nf & 1) {
          auto s0 = __builtin_amdgcn_permlane16_swap(pk[nf - 1][0], pk[nf][0], false, false);
          auto s1 = __builtin_amdgcn_permlane16_swap(pk[nf - 1][1], pk[nf][1], false, false);
          __builtin_amdgcn_raw_buffer_store_b128(u32x4{s0[0], s1[0], s0[1], s1[1]}, rsC, co + (g & 1) * 4096 + (g >> 1) * 16 + (nf - 1) * 4096, 0, STAUX3);
        }
      }
      typedef __attribute__((ext_vector_type(2))) unsigned u32x2;
      __builtin_amdgcn_raw_buffer_store_b64(u32x2{pk[NF - 1][0], pk[NF - 1][1]}, rsC, co + g * 8 + (NF - 1) * 4096, 0, STAUX3);
    }
  }
#endif
}

template <int MODE> constexpr int lds_bytes() {
  return Cfg<MODE>::NS * (BM * BK * 2 + BK * Cfg<MODE>::BN * 2) + (MODE == 3 ? 3072 : MODE == 4 ? 0 : 40960 + 16384);
}

extern "C" void kernel_launch(void* const* d_in, const int* in_sizes, int n_in, void* d_out, int out_size, void* d_ws,
                              size_t ws_size, hipStream_t stream) {
  const u16* x = (const u16*)d_in[0];
  const u16* w1 = (const u16*)d_in[1];
  const u16* w2 = (const u16*)d_in[2];
  const u16* w3 = (const u16*)d_in[3];
  const u16* w4 = (const u16*)d_in[4];
  char* ws = (char*)d_ws;
  u16* hs = (u16*)(ws + WS_HS);
  u16* x1 = (u16*)(ws + WS_X1);
  u16* h2 = (u16*)(ws + WS_H2);
  float* st = (float*)(ws + WS_ST);
  float* cs = (float*)(ws + WS_CS);
  (void)hipFuncSetAttribute((const void*)gemm_k<1>, hipFuncAttributeMaxDynamicSharedMemorySize, lds_bytes<1>());
  (void)hipFuncSetAttribute((const void*)gemm_k<2>, hipFuncAttributeMaxDynamicSharedMemorySize, lds_bytes<2>());
  (void)hipFuncSetAttribute((const void*)gemm_k<3>, hipFuncAttributeMaxDynamicSharedMemorySize, lds_bytes<3>());
  (void)hipFuncSetAttribute((const void*)gemm_k<4>, hipFuncAttributeMaxDynamicSharedMemorySize, lds_bytes<4>());
  gemm_k<1><<<256, 512, lds_bytes<1>(), stream>>>(x, w1, hs, x, st, cs, w3, w2);
  gemm_k<2><<<256, 512, lds_bytes<2>(), stream>>>(hs, w2, x1, x, st, cs, w3, w2);
  gemm_k<3><<<256, 512, lds_bytes<3>(), stream>>>(x1, w3, h2, x, st, cs, w3, w4);
  gemm_k<4><<<256, 512, lds_bytes<4>(), stream>>>(h2, w4, (u16*)d_out, x1, st, cs, w3, w4);
}
```
